```python
import torch

def _softmax(x, axis=-1):
    return torch.softmax(x, dim=axis)
def _gelu(x):
    return torch.nn.functional.gelu(x, approximate="tanh")
def _silu(x):
    return torch.nn.functional.silu(x)
def _sigmoid(x):
    return torch.sigmoid(x)
def _layernorm(x, axis=-1, eps=1e-5):
    m = x.mean(dim=axis, keepdim=True)
    v = x.var(dim=axis, keepdim=True, unbiased=False)
    return (x - m) / torch.sqrt(v + eps)
def _scatter_add(tgt, idx, src, axis=0):
    return tgt.clone().index_add_(axis, idx, src)
def _segment_sum(src, seg, n):
    out = torch.zeros((n,) + src.shape[1:], dtype=src.dtype)
    return out.index_add_(0, seg, src)
def _pad(x, pw):
    flat = tuple(v for pair in reversed(pw) for v in pair)
    return torch.nn.functional.pad(x, flat)

kernel_name = 'concat_heads__ssm_conv_s4d__linear_attn_block__prompt'

def setup_inputs():

    _w = (lambda a: (lambda t: t.to(torch.bfloat16) if t.is_floating_point() else t)(torch.as_tensor(a)))
    def _A_setup():
        Bsz, L, D, N = 1, 2048, 576, 128
        return {
            "u": _w((lambda *s: torch.randn(*s, dtype=torch.bfloat16) * 0.05)(Bsz, L, D)),
            "log_A_real": _w((lambda *s: torch.randn(*s, dtype=torch.bfloat16) * 6.4)(D, N) * 0.5),
            "A_imag": _w((lambda *s: torch.randn(*s, dtype=torch.bfloat16) * 0.05)(D, N) * 3.14),
            "C_real": _w((lambda *s: torch.randn(*s, dtype=torch.bfloat16) * 0.1)(D, N) / N ** 0.5),
            "C_imag": _w((lambda *s: torch.randn(*s, dtype=torch.bfloat16) * 0.1)(D, N) / N ** 0.5),
            "log_dt": _w((lambda *s: torch.randn(*s, dtype=torch.bfloat16) * 0.05)(D) * 0.5 - 4.0),
            "D_skip": _w((lambda *s: torch.randn(*s, dtype=torch.bfloat16) * 0.05)(D)),
        }
    _A_in = _A_setup()

    def _B_setup():
        B, S, D = 1, 2048, 576
        return {
            "x": _w((lambda *s: torch.randn(*s, dtype=torch.bfloat16) * 0.05)(B, S, D)),
            "wqkv": _w((lambda *s: torch.randn(*s, dtype=torch.bfloat16) * 0.4)(D, 3 * D)),
            "wo": _w((lambda *s: torch.randn(*s, dtype=torch.bfloat16) * 0.05)(D, D)),
        }
    _B_in = _B_setup()

    return {
        "A_u": _A_in["u"],
        "A_log_A_real": _A_in["log_A_real"],
        "A_A_imag": _A_in["A_imag"],
        "A_C_real": _A_in["C_real"],
        "A_C_imag": _A_in["C_imag"],
        "A_log_dt": _A_in["log_dt"],
        "A_D_skip": _A_in["D_skip"],
        "B_x": _B_in["x"],
        "B_wqkv": _B_in["wqkv"],
        "B_wo": _B_in["wo"],
    }

def reference(A_u, A_log_A_real, A_A_imag, A_C_real, A_C_imag, A_log_dt, A_D_skip, B_x, B_wqkv, B_wo):
    def _A_ref(u, log_A_real, A_imag, C_real, C_imag, log_dt, D_skip):
        L = u.shape[1]
        dt = torch.exp(log_dt)
        A = -torch.exp(log_A_real) + 1j * A_imag
        C = (C_real + 1j * C_imag) * dt[:, None]
        pos = (lambda x, dt: x.to(dt))(torch.arange(L), log_dt.dtype)
        P = torch.exp((A * dt[:, None])[:, :, None] * pos[None, None, :])
        K = 2.0 * torch.einsum("dn,dnl->dl", C, P).real
        L2 = 2 * L
        u_t = (lambda x, *d: x.permute(*d))(u, 0, 2, 1)
        Kf = (lambda x, *a, **k: torch.fft.rfft(x.float(), *a, **k))(K, L2)
        Uf = (lambda x, *a, **k: torch.fft.rfft(x.float(), *a, **k))(u_t, L2)
        y = (lambda x, *a, **k: torch.fft.irfft(x, *a, **k).to(torch.bfloat16))(Uf * Kf[None, :, :], L2)[:, :, :L]
        y = (lambda x, *d: x.permute(*d))(y, 0, 2, 1)
        return y + u * D_skip[None, None, :]

    def _B_ref(x, wqkv, wo):
        B, S, D = x.shape
        H = 32; Dh = D // H
        qkv = (lambda x, *d: x.permute(*d))((x @ wqkv).reshape(B, S, 3, H, Dh), 2, 0, 3, 1, 4)
        q, k, v = qkv[0], qkv[1], qkv[2]
        phi = lambda t: torch.where(t > 0, t + 1, torch.exp(t)) + 1e-6
        pq, pk = phi(q), phi(k)
        kv = torch.einsum("bhsd,bhse->bhde", pk, v)
        z = torch.einsum("bhsd->bhd", pk)
        num = torch.einsum("bhqd,bhde->bhqe", pq, kv)
        den = torch.einsum("bhqd,bhd->bhq", pq, z)[..., None] + 1e-6
        o = num / den
        return (lambda x, *d: x.permute(*d))(o, 0, 2, 1, 3).reshape(B, S, D) @ wo

    a = _A_ref(A_u, A_log_A_real, A_A_imag, A_C_real, A_C_imag, A_log_dt, A_D_skip)
    b = _B_ref(B_x, B_wqkv, B_wo)
    return torch.concatenate([a, b], axis=-1)
```

```cpp
#include <hip/hip_runtime.h>
#include <hip/hip_bf16.h>
#include <stdint.h>

typedef unsigned short u16;
static __device__ __forceinline__ float bf2f(u16 x) { return __uint_as_float(((unsigned)x) << 16); }
static __device__ __forceinline__ u16 f2bf(float f) {
    unsigned u = __float_as_uint(f);
    if ((u & 0x7fffffffu) > 0x7f800000u) return (u16)((u >> 16) | 0x40);
    return (u16)((u + 0x7fffu + ((u >> 16) & 1u)) >> 16);
}
static __device__ __forceinline__ float rbf(float f) { return bf2f(f2bf(f)); }

constexpr int L = 2048, D = 576, N = 128, H = 32, DH = 18;

__global__ void nv_kgen(const u16* logA, const u16* Aim, const u16* Cre, const u16* Cim, const u16* logdt, float* K) {
    int d = blockIdx.x;
    __shared__ float zr[N], zi[N], cr[N], ci[N];
    float dt = rbf(expf(bf2f(logdt[d])));
    if (threadIdx.x < N) {
        int n = threadIdx.x;
        float ar = -rbf(expf(bf2f(logA[d * N + n])));
        float ai = bf2f(Aim[d * N + n]);
        zr[n] = ar * dt; zi[n] = ai * dt;
        cr[n] = bf2f(Cre[d * N + n]) * dt; ci[n] = bf2f(Cim[d * N + n]) * dt;
    }
    __syncthreads();
    for (int l = threadIdx.x; l < L; l += blockDim.x) {
        float pos = rbf((float)l);
        float acc = 0.f;
        for (int n = 0; n < N; ++n) {
            float e = expf(zr[n] * pos);
            float s, c; sincosf(zi[n] * pos, &s, &c);
            float pr = e * c, pi = e * s;
            acc += cr[n] * pr - ci[n] * pi;
        }
        K[d * L + l] = 2.0f * acc;
    }
}
__global__ void nv_conv(const u16* u, const float* K, const u16* Dskip, u16* out) {
    int d = blockIdx.x;
    __shared__ float su[L], sk[L];
    for (int l = threadIdx.x; l < L; l += blockDim.x) { su[l] = bf2f(u[l * D + d]); sk[l] = K[d * L + l]; }
    __syncthreads();
    float dsk = bf2f(Dskip[d]);
    for (int l = threadIdx.x; l < L; l += blockDim.x) {
        float acc = 0.f;
        for (int j = 0; j <= l; ++j) acc += sk[j] * su[l - j];
        float y = rbf(acc);
        float sk2 = rbf(su[l] * dsk);
        out[(size_t)l * (2 * D) + d] = f2bf(y + sk2);
    }
}
__global__ void nv_gemm(const u16* A, const u16* B, u16* C, int M, int Nn, int Kd, int ldc) {
    int col = blockIdx.x * blockDim.x + threadIdx.x, row = blockIdx.y;
    if (col >= Nn) return;
    float acc = 0.f;
    for (int k = 0; k < Kd; ++k) acc += bf2f(A[row * Kd + k]) * bf2f(B[k * Nn + col]);
    C[(size_t)row * ldc + col] = f2bf(acc);
}
static __device__ __forceinline__ float phi(float t) {
    float a = t > 0.f ? rbf(t + 1.0f) : rbf(expf(t));
    return rbf(a + 1e-6f);
}
__global__ void nv_kv(const u16* qkv, float* kv, float* z) {
    int h = blockIdx.x; int t = threadIdx.x;
    if (t < DH * DH) {
        int dd = t / DH, e = t % DH; float acc = 0.f;
        for (int s = 0; s < L; ++s) acc += phi(bf2f(qkv[s * 3 * D + D + h * DH + dd])) * bf2f(qkv[s * 3 * D + 2 * D + h * DH + e]);
        kv[(h * DH + dd) * DH + e] = rbf(acc);
    } else if (t < DH * DH + DH) {
        int dd = t - DH * DH; float acc = 0.f;
        for (int s = 0; s < L; ++s) acc += phi(bf2f(qkv[s * 3 * D + D + h * DH + dd]));
        z[h * DH + dd] = rbf(acc);
    }
}
__global__ void nv_o(const u16* qkv, const float* kv, const float* z, u16* o) {
    int s = blockIdx.x; int c = threadIdx.x; if (c >= D) return;
    int h = c / DH, e = c % DH;
    float num = 0.f, den = 0.f;
    for (int dd = 0; dd < DH; ++dd) {
        float pq = phi(bf2f(qkv[s * 3 * D + h * DH + dd]));
        num += pq * kv[(h * DH + dd) * DH + e];
        den += pq * z[h * DH + dd];
    }
    num = rbf(num); den = rbf(rbf(den) + 1e-6f);
    o[s * D + c] = f2bf(num / den);
}

extern "C" void kernel_launch(void* const* d_in, const int* in_sizes, int n_in,
                              void* d_out, int out_size, void* d_ws, size_t ws_size,
                              hipStream_t stream) {
    const u16* u = (const u16*)d_in[0]; const u16* logA = (const u16*)d_in[1]; const u16* Aim = (const u16*)d_in[2];
    const u16* Cre = (const u16*)d_in[3]; const u16* Cim = (const u16*)d_in[4]; const u16* logdt = (const u16*)d_in[5];
    const u16* Dskip = (const u16*)d_in[6]; const u16* x = (const u16*)d_in[7]; const u16* wqkv = (const u16*)d_in[8];
    const u16* wo = (const u16*)d_in[9];
    u16* out = (u16*)d_out;
    char* ws = (char*)d_ws;
    float* K = (float*)ws; ws += (size_t)D * L * 4;
    u16* qkv = (u16*)ws; ws += (size_t)L * 3 * D * 2;
    float* kv = (float*)ws; ws += (size_t)H * DH * DH * 4;
    float* z = (float*)ws; ws += (size_t)H * DH * 4 + 256;
    u16* o = (u16*)ws;
    nv_kgen<<<D, 256, 0, stream>>>(logA, Aim, Cre, Cim, logdt, K);
    nv_conv<<<D, 256, 0, stream>>>(u, K, Dskip, out);
    nv_gemm<<<dim3((3 * D + 255) / 256, L), 256, 0, stream>>>(x, wqkv, qkv, L, 3 * D, D, 3 * D);
    nv_kv<<<H, 384, 0, stream>>>(qkv, kv, z);
    nv_o<<<L, 576, 0, stream>>>(qkv, kv, z, o);
    nv_gemm<<<dim3((D + 255) / 256, L), 256, 0, stream>>>(o, wo, out + D, L, D, D, 2 * D);
}
```

```cpp
#include <hip/hip_runtime.h>
#include <stdint.h>
typedef unsigned short u16;
typedef unsigned int u32;
typedef unsigned long long u64;
#define LAS __attribute__((address_space(3)))
#define GAS __attribute__((address_space(1)))
#define DI __device__ __forceinline__
typedef short s16x4 __attribute__((ext_vector_type(4)));
typedef short s16x8 __attribute__((ext_vector_type(8)));
typedef __bf16 bf16x8 __attribute__((ext_vector_type(8)));
typedef __bf16 bf16x2 __attribute__((ext_vector_type(2)));
typedef _Float16 h16x8 __attribute__((ext_vector_type(8)));
typedef _Float16 f16x2 __attribute__((ext_vector_type(2)));
typedef float f32x2 __attribute__((ext_vector_type(2)));
typedef float f32x4 __attribute__((ext_vector_type(4)));
typedef u32 u32x2 __attribute__((ext_vector_type(2)));
typedef u32 u32x3 __attribute__((ext_vector_type(3)));
typedef u32 u32x4 __attribute__((ext_vector_type(4)));

#define PF_DELAY1 3
#define PF_DELAY2 1
#define U_LATE 1
constexpr int L = 2048, D = 576;
constexpr int LDS_B = 163840;
constexpr u32 WS_EPOCH = 0;
constexpr u32 WS_STARTED = 4096;
constexpr u32 WS_PART = 65536;
constexpr u32 WS_RED = 1u << 20;
constexpr u32 WS_OFLAG = (1u << 20) + (1u << 18);
constexpr u32 WS_OF = 2u << 20;
constexpr u32 WS_SPAN = 8u << 20;
constexpr size_t WS_STAMP = 64u << 20;
constexpr u32 OOB = 0x80000000u;

struct Params {
    const u16 *u, *logA, *Aim, *Cre, *Cim, *logdt, *Dskip, *x, *wqkv, *wo;
    u16* out;
    char* ws;
};

DI float bf2f(u16 x) { return __uint_as_float(((u32)x) << 16); }
DI u32 cvtpk(float lo, float hi) { f32x2 v = {lo, hi}; bf16x2 p = __builtin_convertvector(v, bf16x2); return __builtin_bit_cast(u32, p); }
DI float lo16(u32 p) { return __uint_as_float(p << 16); }
DI float hi16(u32 p) { return __uint_as_float(p & 0xffff0000u); }
DI float phi1(float t) {
    float e = __builtin_amdgcn_fmed3f(__builtin_amdgcn_exp2f(t * 1.44269504088896341f), 0.f, 1.f);
    return e + (fmaxf(t, 0.f) + 1e-6f);
}
DI u32 phi2(float a, float b) { return cvtpk(phi1(a), phi1(b)); }
DI s16x4 trread(const char* p) { return __builtin_amdgcn_ds_read_tr16_b64_v4i16((LAS s16x4*)p); }
DI bf16x8 cat8(s16x4 a, s16x4 b) { return __builtin_bit_cast(bf16x8, __builtin_shufflevector(a, b, 0, 1, 2, 3, 4, 5, 6, 7)); }
#define MFMA32(a, b, c) __builtin_amdgcn_mfma_f32_16x16x32_bf16((a), (b), (c), 0, 0, 0)
#define MFMA16(a, b, c) __builtin_amdgcn_mfma_f32_16x16x16bf16_1k((a), (b), (c), 0, 0, 0)
#define MFMAH(a, b, c) __builtin_amdgcn_mfma_f32_16x16x32_f16((a), (b), (c), 0, 0, 0)
#define BAR() __builtin_amdgcn_s_barrier()
#define SB() __builtin_amdgcn_sched_barrier(0)
#define WAITVM(n) asm volatile("s_waitcnt vmcnt(" #n ")" ::: "memory")
#define WAITLG() asm volatile("s_waitcnt lgkmcnt(0)" ::: "memory")
#define RLX __ATOMIC_RELAXED, __HIP_MEMORY_SCOPE_AGENT
constexpr int SPIN_CAP = 1 << 17;

#define ST_ARG
#define ST_PASS
#define STAMP(k)
#define STAMPH(k)

DI void dma16(u32 ldsaddr, u32 voff, __amdgpu_buffer_rsrc_t rs, u32 soff) {
    asm volatile("s_mov_b32 m0, %0\n\ts_nop 0\n\tbuffer_load_dwordx4 %1, %2, %3 offen lds" :: "s"(ldsaddr), "v"(voff), "s"(rs), "s"(soff) : "memory");
}
DI __amdgpu_buffer_rsrc_t mkrs(const void* p, u32 bytes) { return __builtin_amdgcn_make_buffer_rsrc((void*)p, 0, bytes, 0x00020000); }
DI u32 load_epoch(const Params& p) { return __hip_atomic_load((GAS u32*)(p.ws + WS_EPOCH), RLX); }
DI u32 mk_tag(u32 epoch) { return __builtin_amdgcn_readfirstlane(epoch) + 1u; }

constexpr int XST = 16384;
constexpr int WSEC = 12288;
constexpr int STG = 40960;
constexpr int IMG0 = STG + XST + WSEC;
constexpr int PQ0 = 2 * STG + XST + WSEC;
constexpr int PQ1 = 3 * STG + XST + WSEC;
constexpr int KVS = 3 * STG;
constexpr int ZM = KVS + 3072;
constexpr int XCH = PQ0;

struct BTab { u32 v[8][64][20]; };
constexpr BTab mk_btab() {
    BTab t{};
    for (int w = 0; w < 8; ++w)
        for (int lane = 0; lane < 64; ++lane) {
            const int q = lane >> 4, c16 = lane & 15;
            u32* o = t.v[w][lane];
            for (int k = 0; k < 2; ++k) {
                const int tau = w + 8 * k;
                const u64 PA = 0x4433322211100ull, PB = 0x4343232121010ull;
                int a = 0, bb = -1;
                if (tau < 13) { a = (int)((PA >> (4 * tau)) & 15); bb = (int)((PB >> (4 * tau)) & 15); } else { a = tau - 13; }
                const int ra = 16 * a + c16;
                int rb = bb >= 0 ? 72 + 16 * bb + c16 : 72; rb = rb > 143 ? 143 : rb;
                o[3 * k + 0] = IMG0 + (ra / 48) * STG + (ra % 48) * 256 + ((q ^ (ra & 15)) << 4);
                o[3 * k + 1] = IMG0 + (rb / 48) * STG + (rb % 48) * 256 + ((q ^ (rb & 15)) << 4);
                const int gd = 4 * a + q;
                u32 off = OOB;
                if (tau < 13) { const int e = 16 * bb + c16, j = gd - ((18 * (e / 18)) >> 2); if (e < 72 && j >= 0 && j <= 4) off = (e * 5 + j) * 16; }
                else if (c16 == 0 && gd < 18) off = (360 + gd) * 16;
                o[3 * k + 2] = off;
            }
            {
                const int a = w < 2 ? 3 + w : 4, ra = 16 * a + c16, gd = 4 * a + q;
                o[6] = IMG0 + (ra / 48) * STG + (ra % 48) * 256 + ((q ^ (ra & 15)) << 4);
            }
            int n = 7;
            for (int bb = 0; bb < 5; ++bb) {
                const int e = 16 * bb + c16, first = (18 * (e / 18)) >> 2;
                for (int a = 0; a < 5; ++a) {
                    if (a < bb - 1 || a > bb + 1) continue;
                    const int j = 4 * a + q - first;
                    o[n++] = KVS + ((e < 72 && j >= 0 && j <= 4) ? e * 5 + j : 378) * 8;
                }
            }
        }
    return t;
}
__device__ const BTab BTAB = mk_btab();

template <int NT> struct Frags { bf16x8 a[2][2]; bf16x8 b[2][NT]; };
struct Addr { u32 ax0, ax1, ke, ko, ve, vo, mx; };
template <int MODE, int CH> struct TileSet;
template <int CH> struct TileSet<0, CH> { static constexpr int NT = CH == 0 ? 5 : 4; static constexpr int T(int t) { return CH == 0 ? (t == 0 ? 0 : t == 1 ? 1 : t == 2 ? 4 : t == 3 ? 5 : 6) : (t == 0 ? 2 : t == 1 ? 3 : t == 2 ? 7 : 8); } };
template <int CH> struct TileSet<1, CH> { static constexpr int NT = CH == 0 ? 3 : 2; static constexpr int T(int t) { return CH == 0 ? t : 3 + t; } };

template <int MODE, int CH>
DI bf16x8 read_b(const char* sw, const Addr& A, int ks, int t) {
    const int T = TileSet<MODE, CH>::T(t);
    const char* pb;
    if (MODE == 0 && T == 4) pb = sw + A.mx;
    else if (MODE == 0 && T > 4) pb = sw + (((T - 5) & 1) ? A.vo : A.ve) + 64 * ((T - 5) >> 1);
    else pb = sw + ((T & 1) ? A.ko : A.ke) + 64 * (T >> 1);
    pb += ks * 6144;
    return cat8(trread(pb), trread(pb + 768));
}
template <int MODE, int CH>
DI void read_all(const char* lds, int slot, const Addr& A, Frags<TileSet<MODE, CH>::NT>& F) {
    const char* sx = lds + slot * STG; const char* sw = sx + XST;
#pragma unroll
    for (int ks = 0; ks < 2; ++ks) {
#pragma unroll
        for (int rt = 0; rt < 2; ++rt) F.a[ks][rt] = *(const bf16x8*)(sx + (ks ? A.ax1 : A.ax0) + rt * 2048);
#pragma unroll
        for (int t = 0; t < TileSet<MODE, CH>::NT; ++t) F.b[ks][t] = read_b<MODE, CH>(sw, A, ks, t);
    }
}
template <int MC, int MN, int CH, int RD>
DI void iter(const char* lds, const Addr& A, int rslot, Frags<TileSet<MC, CH>::NT>& C, Frags<TileSet<MN, CH>::NT>& N, f32x4 (&acc)[2][5]) {
    constexpr int NTC = TileSet<MC, CH>::NT, NTN = TileSet<MN, CH>::NT;
    constexpr int NP = 2 * NTC, NF = 4 + 2 * NTN;
    const char* sx = lds + rslot * STG; const char* sw = sx + XST;
#pragma unroll
    for (int j = 0; j < NP; ++j) {
        if (RD) {
#pragma unroll
            for (int i = (j * NF) / NP; i < ((j + 1) * NF) / NP; ++i) {
                const int ks = i / (2 + NTN), r = i % (2 + NTN);
                if (r < 2) N.a[ks][r] = *(const bf16x8*)(sx + (ks ? A.ax1 : A.ax0) + r * 2048);
                else N.b[ks][r - 2] = read_b<MN, CH>(sw, A, ks, r - 2);
            }
        }
        const int ks = j / NTC, t = j % NTC;
#pragma unroll
        for (int rt = 0; rt < 2; ++rt) {
            if (MC == 0) acc[rt][t] = MFMA32(C.a[ks][rt], C.b[ks][t], acc[rt][t]);
            else acc[rt][t] = MFMA32(C.b[ks][t], C.a[ks][rt], acc[rt][t]);
        }
        SB();
    }
}

DI void wo_dma(const Params& p, u32 ldsbase, int cg, int first, int step, int lane) {
    const __amdgpu_buffer_rsrc_t rw = mkrs(p.wo, D * D * 2);
    u32 v[3];
#pragma unroll
    for (int i = 0; i < 3; ++i) { int pos = 64 * i + lane, k = pos / 12, c = (pos % 12) ^ (2 * ((k >> 2) & 1)); v[i] = k * 1152 + c * 16 + ((144 * cg) & ~63); }
#pragma unroll 1
    for (int m = first; m < 36; m += step) {
#pragma unroll
        for (int i = 0; i < 3; ++i) dma16(ldsbase + m * 3072 + i * 1024, v[i], rw, m * (16 * 1152));
    }
}

struct Ldr { __amdgpu_buffer_rsrc_t rx, rw; u32 vx0, vx1, vw[3]; };
template <int NSEC>
DI void ld_stage(const Ldr& R, u32 ldsbase, int slot, int st, int sec0, int h) {
    const u32 base = ldsbase + slot * STG;
#pragma unroll
    for (int i = 0; i < 4; ++i) dma16(base + (32 * h + 8 * i) * 128, (i & 1) ? R.vx1 : R.vx0, R.rx, st * 128 + (i >> 1) * (16 * 1152));
#pragma unroll
    for (int sec = 0; sec < NSEC; ++sec)
#pragma unroll
        for (int i = 0; i < 3; ++i) dma16(base + XST + sec * WSEC + h * 3072 + i * 1024, R.vw[i], R.rw, st * (64 * 3456) + sec0 + sec * 1152);
}

DI void b_helper(const Params& p, char* lds, const int h, const int g, const int it, const int cg, u32& tagv ST_ARG) {
    const int lane = threadIdx.x & 63;
    const u32 ldsbase = (u32)(uintptr_t)(LAS char*)lds;
    __builtin_amdgcn_s_setprio(3);
    Ldr R;
    R.rx = mkrs(p.x, L * D * 2);
    R.rw = mkrs(p.wqkv, D * 3 * D * 2);
    {
        int row0 = it * 128 + 32 * h + (lane >> 3);
        R.vx0 = row0 * 1152 + (((lane & 7) ^ (lane >> 4)) << 4);
        R.vx1 = (row0 + 8) * 1152 + (((lane & 7) ^ (4 + (lane >> 4))) << 4);
#pragma unroll
        for (int i = 0; i < 3; ++i) { int pos = 64 * i + lane, k = pos / 12, c = (pos % 12) ^ (2 * ((k >> 3) & 1)); R.vw[i] = (16 * h + k) * 3456 + c * 16 + ((144 * g) & ~63); }
    }
    STAMPH(12);
    ld_stage<2>(R, ldsbase, 0, 0, 1152, h);
    STAMPH(13);
    ld_stage<2>(R, ldsbase, 1, 1, 1152, h);
    STAMPH(14);
    WAITVM(10);
    STAMPH(15);
    BAR();
    ld_stage<2>(R, ldsbase, 2, 2, 1152, h);
    asm volatile("" ::: "memory");
    tagv = load_epoch(p);
    asm volatile("" ::: "memory");
    WAITVM(11); BAR();
#pragma unroll 1
    for (int s = 0; s < 6; ++s) { ld_stage<2>(R, ldsbase, (s + 3) & 3, s + 3, 1152, h); WAITVM(10); BAR(); }
#pragma unroll 1
    for (int s = 6; s < 9; ++s) { ld_stage<1>(R, ldsbase, (s + 3) & 3, s - 6, 0, h); WAITVM(7); BAR(); }
    BAR();
#pragma unroll 1
    for (int s = 0; s < 6; ++s) { ld_stage<1>(R, ldsbase, (s + 12) & 3, s + 3, 0, h); WAITVM(7); BAR(); }
    WAITVM(0); BAR();
    STAMPH(8);
    {
        const u32 tag = mk_tag(tagv);
        const __amdgpu_buffer_rsrc_t rs = mkrs(p.ws, WS_SPAN);
        const int u0 = h * 64 + lane, u1 = u0 + 256;
        const u32 o0 = WS_RED + g * 6144 + u0 * 16, o1 = u1 < 378 ? o0 + 4096 : o0;
        u32x4 a, b;
        for (int spin = 0; spin < SPIN_CAP; ++spin) {
            asm volatile("" ::: "memory");
            a = __builtin_amdgcn_raw_buffer_load_b128(rs, o0, 0, 16);
            b = __builtin_amdgcn_raw_buffer_load_b128(rs, o1, 0, 16);
            if (__all(a[1] == tag && a[3] == tag && b[1] == tag && b[3] == tag)) break;
        }
        *(u32x2*)(lds + KVS + u0 * 8) = (u32x2){a[0], a[2]};
        if (u1 < 360) *(u32x2*)(lds + KVS + u1 * 8) = (u32x2){b[0], b[2]};
        else if (u1 < 378) {
            const int d0 = 4 * (u1 - 360);
#pragma unroll
            for (int hh = 0; hh < 4; ++hh) {
                u32 m0 = ((d0 / 18 == hh) ? 0xffffu : 0u) | (((d0 + 1) / 18 == hh) ? 0xffff0000u : 0u);
                u32 m1 = (((d0 + 2) / 18 == hh) ? 0xffffu : 0u) | (((d0 + 3) / 18 == hh) ? 0xffff0000u : 0u);
                *(u32x2*)(lds + ZM + hh * 160 + d0 * 2) = (u32x2){b[0] & m0, b[2] & m1};
            }
        } else if (u1 < 382) *(u32x4*)(lds + ZM + (u1 - 378) * 160 + 144) = (u32x4){0, 0, 0, 0};
        else if (u1 == 382) *(u32x2*)(lds + KVS + 378 * 8) = (u32x2){0, 0};
    }
    STAMPH(9);
    WAITLG(); BAR();
    wo_dma(p, ldsbase, cg, h, 4, lane);
    WAITVM(0); BAR();
}

template <int CH>
DI void b_compute(const Params& p, char* lds, const int w, const int g, const int it, u32& tagv ST_ARG) {
    const int lane = threadIdx.x & 63;
    const int rg = w & 3;
    const int q = lane >> 4, c16 = lane & 15;
    const int delta = (144 * g) & 63;
    Addr A;
    {
        int row = 32 * rg + c16;
        int sw = (row >> 1) & 7;
        A.ax0 = row * 128 + ((q ^ sw) << 4);
        A.ax1 = row * 128 + (((4 + q) ^ sw) << 4);
        const int rp = c16 >> 2, pp = c16 & 3, b2 = 2 * (q & 1), c0 = (delta >> 4) + (pp >> 1);
        const int rb = 192 * (8 * q + rp) + 8 * (pp & 1);
        A.ke = rb + 16 * (c0 ^ b2); A.ko = rb + 16 * ((c0 + 2) ^ b2);
        A.ve = WSEC + rb + 16 * ((c0 + 1) ^ b2); A.vo = WSEC + rb + 16 * ((c0 + 3) ^ b2);
        A.mx = rb + (pp < 2 ? 16 * (((delta >> 4) + 8) ^ b2) : WSEC + 16 * ((delta >> 4) ^ b2));
    }
    f32x4 acc[2][5];
#pragma unroll
    for (int i = 0; i < 2; ++i)
#pragma unroll
        for (int j = 0; j < 5; ++j) acc[i][j] = (f32x4){0.f, 0.f, 0.f, 0.f};
    Frags<TileSet<0, CH>::NT> F0, F1;
    Frags<TileSet<1, CH>::NT> G0, G1;
    BAR();
    STAMP(1);
    tagv = load_epoch(p);
    u32x4 bt[5];
#pragma unroll
    for (int k = 0; k < 5; ++k) bt[k] = *(const u32x4*)&BTAB.v[w][lane][4 * k];
    asm volatile("" ::: "memory");
    read_all<0, CH>(lds, 0, A, F0);
    BAR();
#define KVIT(s, CUR, NXT) do { iter<0, 0, CH, 1>(lds, A, ((s) + 1) & 3, CUR, NXT, acc); BAR(); } while (0)
    KVIT(0, F0, F1); KVIT(1, F1, F0); KVIT(2, F0, F1); KVIT(3, F1, F0); KVIT(4, F0, F1); KVIT(5, F1, F0); KVIT(6, F0, F1); KVIT(7, F1, F0);
    iter<0, 1, CH, 1>(lds, A, 9 & 3, F0, G0, acc); BAR();
    STAMP(2);
    asm volatile("" :: "v"(bt[0]), "v"(bt[1]), "v"(bt[2]), "v"(bt[3]), "v"(bt[4]));
#pragma unroll
    for (int t = 0; t < TileSet<0, CH>::NT; ++t) {
        const int T = TileSet<0, CH>::T(t);
#pragma unroll
        for (int rt = 0; rt < 2; ++rt) {
            const f32x4 v = acc[rt][t];
            u32 p0, p1;
            if (T < 4) { p0 = phi2(v[0], v[1]); p1 = phi2(v[2], v[3]); }
            else if (T == 4) {
                const bool isk = c16 < 8;
                p0 = cvtpk(isk ? phi1(v[0]) : v[0], isk ? phi1(v[1]) : v[1]); p1 = cvtpk(isk ? phi1(v[2]) : v[2], isk ? phi1(v[3]) : v[3]);
            } else { p0 = cvtpk(v[0], v[1]); p1 = cvtpk(v[2], v[3]); }
            const int chunk = 4 * rg + 2 * rt + (q >> 1);
            *(u32x2*)(lds + IMG0 + (T / 3) * STG + ((16 * T) % 48 + c16) * 256 + ((chunk ^ c16) << 4) + 8 * (q & 1)) = (u32x2){p0, p1};
        }
    }
    const u32 tag = mk_tag(tagv);
    WAITLG(); BAR();
    const __amdgpu_buffer_rsrc_t rs = mkrs(p.ws, WS_SPAN);
    {
        const s16x8 ones = {0x3F80, 0x3F80, 0x3F80, 0x3F80, 0x3F80, 0x3F80, 0x3F80, 0x3F80};
        bf16x8 fa[2][4], fb[2][4];
        f32x4 d[2];
#pragma unroll
        for (int kk = 0; kk < 4; ++kk) {
            fa[0][kk] = *(const bf16x8*)(lds + (bt[0][0] ^ (kk << 6))); fb[0][kk] = *(const bf16x8*)(lds + (bt[0][1] ^ (kk << 6)));
            fa[1][kk] = *(const bf16x8*)(lds + (bt[0][3] ^ (kk << 6)));
        }
        if (w < 5) {
#pragma unroll
            for (int kk = 0; kk < 4; ++kk) fb[1][kk] = *(const bf16x8*)(lds + (bt[1][0] ^ (kk << 6)));
        } else {
#pragma unroll
            for (int kk = 0; kk < 4; ++kk) fb[1][kk] = __builtin_bit_cast(bf16x8, ones);
        }
        d[0] = (f32x4){0.f, 0.f, 0.f, 0.f}; d[1] = d[0];
#pragma unroll
        for (int kk = 0; kk < 4; ++kk) { d[0] = MFMA32(fa[0][kk], fb[0][kk], d[0]); d[1] = MFMA32(fa[1][kk], fb[1][kk], d[1]); }
        const u32 pbase = WS_PART + (g * 16 + it) * 6144;
        __builtin_amdgcn_raw_buffer_store_b128((u32x4){cvtpk(d[0][0], d[0][1]), tag, cvtpk(d[0][2], d[0][3]), tag}, rs, bt[0][2], pbase, 16);
        __builtin_amdgcn_raw_buffer_store_b128((u32x4){cvtpk(d[1][0], d[1][1]), tag, cvtpk(d[1][2], d[1][3]), tag}, rs, bt[1][1], pbase, 16);
        if (w < 2) {
            f32x4 e = {0.f, 0.f, 0.f, 0.f};
#pragma unroll
            for (int kk = 0; kk < 4; ++kk) e = MFMA32(*(const bf16x8*)(lds + (bt[1][2] ^ (kk << 6))), __builtin_bit_cast(bf16x8, ones), e);
            const int gd = 4 * (3 + w) + q;
            __builtin_amdgcn_raw_buffer_store_b128((u32x4){cvtpk(e[0], e[1]), tag, cvtpk(e[2], e[3]), tag}, rs, (c16 == 0 && gd < 18) ? (360 + gd) * 16 : OOB, pbase, 16);
        }
    }
    if (w == 0 && lane == 0) __hip_atomic_store((GAS u32*)(p.ws + WS_STARTED + blockIdx.x * 8), tag, RLX);
    STAMP(3);
#pragma unroll
    for (int i = 0; i < 2; ++i)
#pragma unroll
        for (int j = 0; j < 5; ++j) acc[i][j] = (f32x4){0.f, 0.f, 0.f, 0.f};
#define QIT(s, CUR, NXT) do { iter<1, 1, CH, 1>(lds, A, ((s) + 10) & 3, CUR, NXT, acc); BAR(); } while (0)
    QIT(0, G0, G1); QIT(1, G1, G0); QIT(2, G0, G1); QIT(3, G1, G0); QIT(4, G0, G1); QIT(5, G1, G0); QIT(6, G0, G1);
    iter<1, 1, CH, 1>(lds, A, 17 & 3, G1, G0, acc);
    iter<1, 1, CH, 0>(lds, A, 0, G0, G1, acc);
    STAMP(4);
#pragma unroll
    for (int t = 0; t < TileSet<1, CH>::NT; ++t) {
        const int T = TileSet<1, CH>::T(t);
#pragma unroll
        for (int rt = 0; rt < 2; ++rt) {
            const f32x4 v = acc[rt][t];
            u32x2 o = {phi2(v[0], v[1]), phi2(v[2], v[3])};
            if (T == 4 && q >= 2) o = (u32x2){0u, 0u};
            *(u32x2*)(lds + (rg < 2 ? PQ0 : PQ1) + ((32 * rg + 16 * rt + c16) & 63) * 176 + 32 * T + 8 * q) = o;
        }
    }
    WAITLG(); BAR();
    STAMP(5);
    {
        const char* pq_p = lds + (w < 4 ? PQ0 : PQ1) + ((16 * w + c16) & 63) * 176 + 8 * q;
        s16x4 pq[5], zf[5], kf[13];
#pragma unroll
        for (int a = 0; a < 5; ++a) { pq[a] = *(const s16x4*)(pq_p + 32 * a); zf[a] = *(const s16x4*)(lds + ZM + (c16 & 3) * 160 + 32 * a + 8 * q); }
#pragma unroll
        for (int n = 0; n < 13; ++n) kf[n] = *(const s16x4*)(lds + bt[(7 + n) >> 2][(7 + n) & 3]);
        f32x4 den = {0.f, 0.f, 0.f, 0.f};
        f32x4 num[5];
#pragma unroll
        for (int a = 0; a < 5; ++a) den = MFMA16(zf[a], pq[a], den);
        {
            int n = 0;
#pragma unroll
            for (int bb = 0; bb < 5; ++bb) {
                num[bb] = (f32x4){0.f, 0.f, 0.f, 0.f};
#pragma unroll
                for (int a = 0; a < 5; ++a) { if (a < bb - 1 || a > bb + 1) continue; num[bb] = MFMA16(kf[n], pq[a], num[bb]); ++n; }
            }
        }
        float rden[4];
#pragma unroll
        for (int k = 0; k < 4; ++k) rden[k] = __builtin_amdgcn_rcpf(den[k] + 1e-6f);
        u32x2 o[5];
#pragma unroll
        for (int bb = 0; bb < 5; ++bb) {
            float ov[4];
#pragma unroll
            for (int k = 0; k < 4; ++k) {
                const int e0 = 16 * bb + k;
                int h0 = e0 / 18; if (h0 > 3) h0 = 3;
                const int h1 = h0 + 1 > 3 ? 3 : h0 + 1;
                const int qthr = (18 * (h0 + 1) - e0 + 3) / 4;
                ov[k] = num[bb][k] * ((qthr <= 3 && q >= qthr) ? rden[h1] : rden[h0]);
            }
            o[bb] = (u32x2){cvtpk(ov[0], ov[1]), cvtpk(ov[2], ov[3])};
        }
        const u32 of = WS_OF + (it * 8 + w) * (18 * 1024);
        __builtin_amdgcn_raw_buffer_store_b128((u32x4){o[0][0], o[0][1], o[1][0], o[1][1]}, rs, of + (2 * g) * 1024 + lane * 16, 0, 16);
        __builtin_amdgcn_raw_buffer_store_b128((u32x4){o[2][0], o[2][1], o[3][0], o[3][1]}, rs, of + (2 * g + 1) * 1024 + lane * 16, 0, 16);
        __builtin_amdgcn_raw_buffer_store_b64(o[4], rs, q < 2 ? of + (16 + (g >> 2)) * 1024 + (c16 + 16 * (g & 3)) * 16 + 8 * q : OOB, 0, 16);
        WAITVM(0);
        if (lane == 0) __hip_atomic_store((GAS u32*)(p.ws + WS_OFLAG + ((it * 8 + w) * 8 + g) * 4), tag, RLX);
    }
    STAMP(6);
    BAR();
}

DI void b2_tile(const Params& p, char* lds, const int w, const int it, const int cg, const u32 tag ST_ARG) {
    const int lane = threadIdx.x & 63;
    const int q = lane >> 4, c16 = lane & 15;
    const int rp = w & 3, kh = (w >> 2) & 1;
    f32x4 acc[2][5];
#pragma unroll
    for (int i = 0; i < 2; ++i)
#pragma unroll
        for (int j = 0; j < 5; ++j) acc[i][j] = (f32x4){0.f, 0.f, 0.f, 0.f};
    if (w < 8) {
        const __amdgpu_buffer_rsrc_t rs = mkrs(p.ws, WS_SPAN);
        const int rt16 = 8 * it + 2 * rp;
        const int delta = (144 * cg) & 63;
        const int rq = c16 >> 2, pp = c16 & 3, c0 = (delta >> 4) + (pp >> 1);
        const int b2 = 2 * (q & 1);
        const char* base = lds + kh * 55296 + 8 * (pp & 1);
        const char* me = base + 192 * (4 * q + rq) + 16 * (c0 ^ b2);
        const char* mo = base + 192 * (4 * q + rq) + 16 * ((c0 + 2) ^ b2);
        const char* le1 = base + 192 * (72 * q + 64 + rq) + 16 * c0;
        const char* lo1 = base + 192 * (72 * q + 64 + rq) + 16 * (c0 + 2);
        const char* le2 = base + 192 * (72 * q + 68 + rq) + 16 * (c0 ^ 2);
        const char* lo2 = base + 192 * (72 * q + 68 + rq) + 16 * ((c0 + 2) ^ 2);
        auto ldW = [&](int k, int ct) -> bf16x8 {
            if (k < 8) { const char* pa = ((ct & 1) ? mo : me) + 192 * (72 * (k >> 1) + 32 * (k & 1)) + 64 * (ct >> 1); return cat8(trread(pa), trread(pa + 16 * 192)); }
            return cat8(trread(((ct & 1) ? lo1 : le1) + 64 * (ct >> 1)), trread(((ct & 1) ? lo2 : le2) + 64 * (ct >> 1)));
        };
        bf16x8 wf[3][5];
#pragma unroll
        for (int k = 0; k < 2; ++k)
#pragma unroll
            for (int ct = 0; ct < 5; ++ct) wf[k][ct] = ldW(k, ct);
        {
            GAS u32* fl = (GAS u32*)(p.ws + WS_OFLAG + ((rt16 + ((lane >> 2) & 1)) * 8 + 4 * kh + (lane & 3)) * 4);
            for (int spin = 0; spin < SPIN_CAP; ++spin) {
                const u32 f = __hip_atomic_load(fl, RLX);
                if (__all(f == tag)) break;
            }
        }
        asm volatile("" ::: "memory");
        STAMP(10);
        u32x4 of[9][2];
        const u32 ob = WS_OF + rt16 * (18 * 1024) + lane * 16;
#pragma unroll
        for (int k = 0; k < 9; ++k)
#pragma unroll
            for (int rt = 0; rt < 2; ++rt) of[k][rt] = __builtin_amdgcn_raw_buffer_load_b128(rs, ob + rt * (18 * 1024) + (k < 8 ? 8 * kh + k : 16 + kh) * 1024, 0, 16);
        SB();
#pragma unroll
        for (int k = 0; k < 9; ++k) {
#pragma unroll
            for (int ct = 0; ct < 5; ++ct) {
                if (k + 2 < 9) wf[(k + 2) % 3][ct] = ldW(k + 2, ct);
#pragma unroll
                for (int rt = 0; rt < 2; ++rt) acc[rt][ct] = MFMA32(wf[k % 3][ct], __builtin_bit_cast(bf16x8, of[k][rt]), acc[rt][ct]);
                SB();
            }
        }
#pragma unroll
        for (int ct = 0; ct < 5; ++ct) *(f32x4*)(lds + XCH + w * 5120 + ct * 1024 + lane * 16) = kh ? acc[0][ct] : acc[1][ct];
    }
    __syncthreads();
    if (w < 8) {
        u16* orow = p.out + (size_t)(128 * it + 32 * rp + 16 * kh + c16) * (2 * D) + D + 72 * cg + 4 * q;
#pragma unroll
        for (int ct = 0; ct < 5; ++ct) {
            const f32x4 own = kh ? acc[1][ct] : acc[0][ct];
            const f32x4 v = own + *(const f32x4*)(lds + XCH + (w ^ 4) * 5120 + ct * 1024 + lane * 16);
            if (ct < 4 || q < 2) *(u32x2*)(orow + 16 * ct) = (u32x2){cvtpk(v[0], v[1]), cvtpk(v[2], v[3])};
        }
    }
}

DI const char* pf_addr(const Params& p, int xcd, int j, int lane) {
    const char* a = (const char*)p.x;
    if (j < 198) {
        const int st = j / 22, kk = j % 22;
        if (kk < 4) a = (const char*)p.x + (size_t)(256 * xcd + 64 * kk + lane) * 1152 + st * 128;
        else { const int li = (kk - 4) * 64 + lane; a = (const char*)p.wqkv + (size_t)(64 * st + li / 18) * 3456 + 1152 + (li % 18) * 128; }
    } else if (j < 279) { const int li = (j - 198) * 64 + lane; a = (const char*)p.wqkv + (size_t)(li / 9) * 3456 + (li % 9) * 128; }
    else if (j < 360) { const int li = (j - 279) * 64 + lane; a = (const char*)p.wo + (size_t)li * 128; }
    return a;
}
DI void spare_body(const Params& p, char* lds, const int xcd, const int r, const int w, u32& tagv ST_ARG) {
    const int tid = threadIdx.x, lane = tid & 63;
    u32 sink[5];
    {
        const int id = r * 12 + w;
        const int j0 = id < 44 ? id : 400;
        asm volatile("global_load_dword %0, %1, off sc1" : "=v"(sink[0]) : "v"(pf_addr(p, xcd, j0, lane)) : "memory");
        if (id < 10) asm volatile("global_load_dword %0, %1, off sc1" : "=v"(sink[1]) : "v"(pf_addr(p, xcd, 188 + id, lane)) : "memory");
        else sink[1] = 0;
        for (int z = 0; z < PF_DELAY1; ++z) __builtin_amdgcn_s_sleep(32);
#pragma unroll
        for (int k = 2; k < 4; ++k)
            asm volatile("global_load_dword %0, %1, off sc1" : "=v"(sink[k]) : "v"(pf_addr(p, xcd, (k == 2 || id < 33) ? 198 + id + 48 * (k - 2) : 400, lane)) : "memory");
        for (int z = 0; z < PF_DELAY2; ++z) __builtin_amdgcn_s_sleep(32);
        u32 s2[2];
#pragma unroll
        for (int k = 0; k < 2; ++k)
            asm volatile("global_load_dword %0, %1, off sc1" : "=v"(s2[k]) : "v"(pf_addr(p, xcd, (k == 0 || id < 33) ? 279 + id + 48 * k : 400, lane)) : "memory");
        WAITVM(0);
        asm volatile("" :: "v"(s2[0]), "v"(s2[1]));
        sink[4] = 0;
    }
    tagv = load_epoch(p);
    WAITVM(0);
#pragma unroll
    for (int k = 0; k < 5; ++k) asm volatile("" :: "v"(sink[k]));
    const u32 tag = mk_tag(tagv);
    __syncthreads();
    if (tid == 0) __hip_atomic_store((GAS u32*)(p.ws + WS_STARTED + blockIdx.x * 8), tag, RLX);
    STAMP(3);
    const int id = xcd * 4 + r, g = id >> 2, qr = id & 3;
    const __amdgpu_buffer_rsrc_t rs = mkrs(p.ws, WS_SPAN);
    const int ul = tid % 96, sb = tid / 96;
    const int un = 95 * qr + ul;
    const bool valid = ul < 95 && un < 378;
    {
        const u32 o0 = WS_PART + (g * 16 + 2 * sb) * 6144 + (valid ? un : 95 * qr) * 16;
        u32x4 a, b;
        for (int spin = 0; spin < SPIN_CAP; ++spin) {
            asm volatile("" ::: "memory");
            a = __builtin_amdgcn_raw_buffer_load_b128(rs, o0, 0, 16);
            b = __builtin_amdgcn_raw_buffer_load_b128(rs, o0 + 6144, 0, 16);
            if (__all(a[1] == tag && a[3] == tag && b[1] == tag && b[3] == tag)) break;
        }
        *(f32x4*)(lds + (sb * 96 + ul) * 16) = (f32x4){lo16(a[0]) + lo16(b[0]), hi16(a[0]) + hi16(b[0]), lo16(a[2]) + lo16(b[2]), hi16(a[2]) + hi16(b[2])};
    }
    STAMP(4);
    __syncthreads();
    if (tid < 95 && valid) {
        f32x4 s = {0.f, 0.f, 0.f, 0.f};
#pragma unroll
        for (int k = 0; k < 8; ++k) s += *(const f32x4*)(lds + (k * 96 + ul) * 16);
        u32 p0 = cvtpk(s[0], s[1]), p1 = cvtpk(s[2], s[3]);
        if (un < 360) {
            const int e = un / 5, j = un % 5, h = e / 18, d0 = 4 * (((18 * h) >> 2) + j), lo = 18 * h, hi = lo + 18;
            p0 &= ((d0 >= lo && d0 < hi) ? 0xffffu : 0u) | ((d0 + 1 >= lo && d0 + 1 < hi) ? 0xffff0000u : 0u);
            p1 &= ((d0 + 2 >= lo && d0 + 2 < hi) ? 0xffffu : 0u) | ((d0 + 3 >= lo && d0 + 3 < hi) ? 0xffff0000u : 0u);
        }
        __builtin_amdgcn_raw_buffer_store_b128((u32x4){p0, tag, p1, tag}, rs, WS_RED + g * 6144 + un * 16, 0, 16);
    }
    STAMP(5);
    __syncthreads();
}

constexpr int A_KR = 0;
constexpr int A_KRC = 4 * 4160;
constexpr int A_U = 99840;
constexpr int A_UC = 4608;
constexpr int A_TB = 0;
constexpr int A_XD = 127488;
constexpr int A_KP = 145920;
constexpr int A_Y = 127488;

DI f32x2 cmul(f32x2 a, f32x2 b) {
    const f32x2 ay = {a[1], a[1]}, ax = {a[0], a[0]}, bs = {-b[1], b[0]};
    return __builtin_elementwise_fma(ax, b, ay * bs);
}
DI float exp_acc(float x) {
    const float t = x * 1.44269502162933349609f;
    float r = fmaf(x, 1.44269502162933349609f, -t);
    r = fmaf(x, 1.92596299112661746e-8f, r);
    const float e = __builtin_amdgcn_exp2f(t);
    return fmaf(e, r * 0.69314718056f, e);
}
template <int ROW> DI void tw_put8(u32 a, u32 b, u32 c, u32 d, u32 e, u32 f, u32 g, u32 h) {
    asm volatile("ds_write_addtid_b32 %0 offset:%8\n\tds_write_addtid_b32 %1 offset:%9\n\tds_write_addtid_b32 %2 offset:%10\n\tds_write_addtid_b32 %3 offset:%11\n\t"
                 "ds_write_addtid_b32 %4 offset:%12\n\tds_write_addtid_b32 %5 offset:%13\n\tds_write_addtid_b32 %6 offset:%14\n\tds_write_addtid_b32 %7 offset:%15"
                 :: "v"(a), "v"(b), "v"(c), "v"(d), "v"(e), "v"(f), "v"(g), "v"(h),
                    "i"(ROW * 272), "i"((ROW + 1) * 272), "i"((ROW + 2) * 272), "i"((ROW + 3) * 272), "i"((ROW + 4) * 272), "i"((ROW + 5) * 272), "i"((ROW + 6) * 272), "i"((ROW + 7) * 272) : "memory");
}
template <int ROW> DI void tw_put1(u32 v) { asm volatile("ds_write_addtid_b32 %0 offset:%1" :: "v"(v), "i"(ROW * 272) : "memory"); }
DI u32 pkh(float lo, float hi) { f32x2 v = {lo, hi}; f16x2 p = __builtin_convertvector(v, f16x2); return __builtin_bit_cast(u32, p); }
DI u32 pkh2(f32x2 v) { f16x2 p = __builtin_convertvector(v, f16x2); return __builtin_bit_cast(u32, p); }
DI f32x2 cexp_p(float zr2, float zrev, float p) {
    const float m = __builtin_amdgcn_exp2f(zr2 * p);
    const float f = __builtin_amdgcn_fractf(zrev * p);
    return (f32x2){m * __builtin_amdgcn_cosf(f), m * __builtin_amdgcn_sinf(f)};
}
template <int CTRL> DI float dppmax(float v) {
    int t = __builtin_amdgcn_update_dpp(__float_as_int(v), __float_as_int(v), CTRL, 0xf, 0xf, false);
    return fmaxf(v, __int_as_float(t));
}
DI float wave_max(float v) {
    v = dppmax<0xB1>(v); v = dppmax<0x4E>(v); v = dppmax<0x141>(v); v = dppmax<0x140>(v);
    float a = __int_as_float(__builtin_amdgcn_readlane(__float_as_int(v), 0)), b = __int_as_float(__builtin_amdgcn_readlane(__float_as_int(v), 16));
    float c = __int_as_float(__builtin_amdgcn_readlane(__float_as_int(v), 32)), d = __int_as_float(__builtin_amdgcn_readlane(__float_as_int(v), 48));
    return fmaxf(fmaxf(a, b), fmaxf(c, d));
}
DI int kidx(int lag) {
    const u32 b = cvtpk((float)lag, 0.f) & 0xffffu;
    return lag < 128 ? lag : (int)b - 17024;
}
template <int UU>
DI void a_finish(char* lds, int c, int lane, float dsk, f32x4 (&acc)[8]) {
    const int q = lane >> 4, c16 = lane & 15;
    char* xb = lds + A_KR + c * A_KRC;
#pragma unroll
    for (int t = 0; t < 4; ++t) *(f32x4*)(xb + UU * 4096 + t * 1024 + lane * 16) = acc[(1 - UU) * 4 + t];
    __syncthreads();
#pragma unroll
    for (int t = 0; t < 4; ++t) {
        const f32x4 y = acc[UU * 4 + t] + *(const f32x4*)(xb + (1 - UU) * 4096 + t * 1024 + lane * 16);
        const int l = 256 * (4 * UU + t) + 16 * c16 + 4 * q;
        const u32x2 uv = *(const u32x2*)(lds + A_U + c * A_UC + 480 + 2 * l);
        *(u32x2*)(lds + A_Y + c * 4096 + 2 * l) = (u32x2){cvtpk(fmaf(lo16(uv[0]), dsk, y[0]), fmaf(hi16(uv[0]), dsk, y[1])), cvtpk(fmaf(lo16(uv[1]), dsk, y[2]), fmaf(hi16(uv[1]), dsk, y[3]))};
    }
}
DI void a_body(const Params& p, char* lds, const int wg  , const int w, u32& tagv ST_ARG) {
    const int tid = threadIdx.x, lane = tid & 63;
    const int c = w >> 1, uu = w & 1;
    const int d = wg * 6 + c;
    const int q = lane >> 4, c16 = lane & 15;
    const int n = 64 * uu + lane;
    const u16 r_la = p.logA[d * 128 + n], r_ai = p.Aim[d * 128 + n], r_cr = p.Cre[d * 128 + n], r_ci = p.Cim[d * 128 + n];
    const u16 r_dt = p.logdt[d], r_ds = p.Dskip[d];
    asm volatile("" ::: "memory");
    u32 pfsink;
    asm volatile("global_load_dword %0, %1, off sc1" : "=v"(pfsink) : "v"(pf_addr(p, wg / 12, 44 + (wg % 12) * 12 + w, lane)) : "memory");
    u32x3 ur[4];
    u32 gi[2][12];
    {
        const u32 kbase = A_KP + c * 1344;
#pragma unroll
        for (int it = 0; it < 2; ++it) {
            const int L0 = 2051 - 8 * (lane + 64 * (2 * it + uu));
            const float f0 = (float)L0;
#pragma unroll
            for (int i = 0; i < 12; i += 2) {
                float fa = f0 - (float)i, fb = f0 - (float)(i + 1);
                if (it == 0) { fa = fminf(fa, 2047.f); fb = fminf(fb, 2047.f); }
                const u32 pk = cvtpk(fa, fb);
                u32 oa = kbase - 2 * 17024 + 2 * (pk & 0xffffu), ob = kbase - 2 * 17024 + 2 * (pk >> 16);
                if (it == 1) { oa = L0 - i < 128 ? kbase + 2 * (L0 - i) : oa; ob = L0 - i - 1 < 128 ? kbase + 2 * max(L0 - i - 1, 0) : ob; }
                asm volatile("" : "+v"(oa), "+v"(ob));
                gi[it][i] = oa; gi[it][i + 1] = ob;
            }
        }
    }
    STAMP(1);
    const float dt = lo16(cvtpk(exp_acc(bf2f(r_dt)), 0.f));
    const float ar = -lo16(cvtpk(exp_acc(bf2f(r_la)), 0.f));
    const float zr2 = (ar * dt) * 1.44269504088896341f, zrev = (bf2f(r_ai) * dt) * 0.15915494309189535f;
    f32x2 cc = {2.0f * (bf2f(r_cr) * dt), 2.0f * (bf2f(r_ci) * dt)};
    const float mx = wave_max(fmaxf(fabsf(cc[0]), fabsf(cc[1])));
    int ex = __builtin_amdgcn_readfirstlane((__float_as_int(mx) >> 23) & 0xff);
    ex = ex < 1 ? 127 : (ex > 253 ? 253 : ex);
    const float sc = __int_as_float((254 - ex) << 23), isc = __int_as_float(ex << 23);
    cc *= sc;
    const f32x2 w1 = cexp_p(zr2, zrev, 1.f), w8 = cexp_p(zr2, zrev, 8.f), w128 = cexp_p(zr2, zrev, 128.f);
    asm volatile("" ::: "memory");
    {
        const char* ub = (const char*)p.u + (tid < 512 ? (size_t)(4 * tid) * 1152 : (size_t)0) + wg * 12;
#pragma unroll
        for (int i = 0; i < 4; ++i) ur[i] = *(const u32x3*)(ub + i * 1152);
    }
    asm volatile("" ::: "memory");
    tagv = load_epoch(p);
    asm volatile("" ::: "memory");
    const char* tr = lds + A_TB + w * (16 * 272) + c16 * 272 + q * 16;
    asm volatile("s_mov_b32 m0, %0\n\ts_nop 0" :: "s"((u32)(uintptr_t)(LAS char*)lds + A_TB + w * (16 * 272)) : "memory");
    h16x8 eb[4];
    u32 pk[16];
#define PUT16() do { tw_put8<0>(pk[0], pk[1], pk[2], pk[3], pk[4], pk[5], pk[6], pk[7]); tw_put8<8>(pk[8], pk[9], pk[10], pk[11], pk[12], pk[13], pk[14], pk[15]); } while (0)
    {
        const f32x2 w8c = {w8[0], -w8[1]};
        const f32x2 w16c = cmul(w8c, w8c), w32c = cmul(w16c, w16c);
        f32x2 e[4] = {(f32x2){1.f, 0.f}, w8c, w16c, cmul(w8c, w16c)};
#pragma unroll
        for (int s4 = 0; s4 < 4; ++s4) {
#pragma unroll
            for (int i = 0; i < 4; ++i) { pk[4 * s4 + i] = pkh2(e[i]); if (s4 < 3) e[i] = cmul(e[i], w32c); }
        }
        PUT16();
#pragma unroll
        for (int k = 0; k < 4; ++k) eb[k] = *(const h16x8*)(tr + 64 * k);
    }
    f32x4 dk[3];
    f32x2 g[8];
    {
        const f32x2 w2 = cmul(w1, w1), w4 = cmul(w2, w2);
        g[0] = cc; g[1] = cmul(cc, w1); g[2] = cmul(g[0], w2); g[3] = cmul(g[1], w2);
        g[4] = cmul(g[0], w4); g[5] = cmul(g[1], w4); g[6] = cmul(g[2], w4); g[7] = cmul(g[3], w4);
#pragma unroll
        for (int r = 0; r < 8; ++r) { pk[r] = pkh2(g[r]); g[r] = cmul(g[r], w128); pk[8 + r] = pkh2(g[r]); }
        PUT16();
        f32x4 a = {0.f, 0.f, 0.f, 0.f};
#pragma unroll
        for (int k = 0; k < 4; ++k) a = MFMAH(*(const h16x8*)(tr + 64 * k), eb[k], a);
        dk[0] = a;
    }
    {
#pragma unroll
        for (int r = 0; r < 4; ++r) { g[2 * r] = cmul(g[2 * r], w128); pk[r] = pkh2(g[2 * r]); }
#pragma unroll
        for (int r = 0; r < 4; ++r) { g[2 * r] = cmul(g[2 * r], w128); pk[4 + r] = pkh2(g[2 * r]); }
#pragma unroll
        for (int ci = 0; ci < 4; ++ci) { g[0] = cmul(g[0], w128); g[4] = cmul(g[4], w128); pk[8 + 2 * ci] = pkh2(g[0]); pk[9 + 2 * ci] = pkh2(g[4]); }
        PUT16();
        f32x4 a = {0.f, 0.f, 0.f, 0.f};
#pragma unroll
        for (int k = 0; k < 4; ++k) a = MFMAH(*(const h16x8*)(tr + 64 * k), eb[k], a);
        dk[1] = a;
    }
    {
        const f32x2 w256 = cmul(w128, w128);
        f32x2 h0 = cmul(g[0], w128), h1 = cmul(g[0], w256);
#pragma unroll
        for (int i = 0; i < 4; ++i) { pk[2 * i] = pkh2(h0); pk[2 * i + 1] = pkh2(h1); h0 = cmul(h0, w256); if (i < 3) h1 = cmul(h1, w256); }
        pk[8] = pkh2(h0);
        tw_put8<0>(pk[0], pk[1], pk[2], pk[3], pk[4], pk[5], pk[6], pk[7]); tw_put1<8>(pk[8]);
        f32x4 a = {0.f, 0.f, 0.f, 0.f};
#pragma unroll
        for (int k = 0; k < 4; ++k) a = MFMAH(*(const h16x8*)(tr + 64 * k), eb[k], a);
        dk[2] = a;
    }
#pragma unroll
    for (int t = 0; t < 3; ++t) dk[t] *= isc;
    if (uu == 1) {
#pragma unroll
        for (int t = 0; t < 3; ++t) *(f32x4*)(lds + A_XD + c * 3072 + t * 1024 + lane * 16) = dk[t];
    }
    STAMP(2);
    __syncthreads();
    STAMP(12);
    if (uu == 0) {
        u16* kp = (u16*)(lds + A_KP + c * 1344);
#pragma unroll
        for (int t = 0; t < 3; ++t) {
            const f32x4 v = dk[t] + *(const f32x4*)(lds + A_XD + c * 3072 + t * 1024 + lane * 16);
            const u32 p0 = cvtpk(v[0], v[1]), p1 = cvtpk(v[2], v[3]);
            if (t == 0) *(u32x2*)(kp + (q >> 1) * 128 + (q & 1) * 4 + 8 * c16) = (u32x2){p0, p1};
            else if (t == 1) {
                if (q < 2) *(u32x2*)(kp + 256 + 64 * q + 4 * c16) = (u32x2){p0, p1};
                else { const int c0 = 2 * (q - 2); *(u32*)(kp + 384 + 32 * c0 + 2 * c16) = p0; *(u32*)(kp + 384 + 32 * (c0 + 1) + 2 * c16) = p1; }
            } else {
                if (q < 2) { u16* k3 = kp + 512 + 64 * q + c16; k3[0] = (u16)p0; k3[16] = (u16)(p0 >> 16); k3[32] = (u16)p1; k3[48] = (u16)(p1 >> 16); }
                else if (lane == 32) kp[640] = (u16)p0;
            }
        }
    }
    __syncthreads();
    STAMP(13);
    {
        char* kr = lds + A_KR + c * A_KRC;
#pragma unroll
        for (int it = 0; it < 2; ++it) {
            const int m = lane + 64 * (2 * it + uu);
            u32 v[11];
#pragma unroll
            for (int i = 0; i < 11; ++i) v[i] = *(const u16*)(lds + gi[it][i]);
            u32 pk[10];
#pragma unroll
            for (int i = 0; i < 10; ++i) pk[i] = v[i] | (v[i + 1] << 16);
#pragma unroll
            for (int s = 0; s < 4; ++s) *(u32x4*)(kr + s * 4160 + m * 16) = (u32x4){pk[3 - s], pk[5 - s], pk[7 - s], pk[9 - s]};
        }
        if (uu == 1 && lane < 8) {
            const u16* kp = (const u16*)(lds + A_KP + c * 1344);
            const int s = lane & 3, half = lane >> 2;
            u32 e[8];
#pragma unroll
            for (int j = 0; j < 8; ++j) { const int lag = s - 8 * half - j; e[j] = lag >= 0 ? (u32)kp[lag] : 0u; }
            *(u32x4*)(kr + s * 4160 + 4096 + half * 16) = (u32x4){e[0] | (e[1] << 16), e[2] | (e[3] << 16), e[4] | (e[5] << 16), e[6] | (e[7] << 16)};
        }
    }
    asm volatile("" :: "v"(ur[0]), "v"(ur[1]), "v"(ur[2]), "v"(ur[3]));
    asm volatile("" :: "v"(pfsink));
    if (w < 8) {
#pragma unroll
        for (int ch = 0; ch < 6; ++ch) {
            const u32 sel = (ch & 1) ? 0x07060302u : 0x05040100u;
            *(u32x2*)(lds + A_U + ch * A_UC + 480 + tid * 8) = (u32x2){__builtin_amdgcn_perm(ur[1][ch >> 1], ur[0][ch >> 1], sel), __builtin_amdgcn_perm(ur[3][ch >> 1], ur[2][ch >> 1], sel)};
        }
    } else if (w < 11) {
        const int i = tid - 512;
        if (i < 180) *(u32x4*)(lds + A_U + (i / 30) * A_UC + (i % 30) * 16) = (u32x4){0, 0, 0, 0};
    }
    __syncthreads();
    STAMP(3);
    f32x4 acc[8];
#pragma unroll
    for (int t = 0; t < 8; ++t) acc[t] = (f32x4){0.f, 0.f, 0.f, 0.f};
    {
        const char* ka = lds + A_KR + c * A_KRC + (c16 & 3) * 4160 + 2 * (2032 - 4 * (c16 >> 2) + 8 * q) - 3584 - 256 * uu - 192;
        const char* ub = lds + A_U + c * A_UC + 448 + 32 * c16 + 16 * q - 256 * uu - 192;
        bf16x8 fa[3], fb[8];
        auto ldA = [&](int i) { const char* pa = ka + 64 * (3 - (i >> 3)) + 512 * (7 - (i & 7)); return cat8(*(const s16x4*)pa, *(const s16x4*)(pa + 8)); };
        auto ldB = [&](int cl, int m) { return *(const bf16x8*)(ub + 64 * (3 - cl) + 512 * m); };
        fa[0] = ldA(0); fa[1] = ldA(1);
#pragma unroll
        for (int m = 0; m < 8; ++m) fb[m] = ldB(0, m);
#pragma unroll
        for (int i = 0; i < 32; ++i) {
            const int cl = i >> 3, sg = i & 7;
            if (i + 2 < 32) fa[(i + 2) % 3] = ldA(i + 2);
            if (sg > 0 && cl < 3) fb[8 - sg] = ldB(cl + 1, 8 - sg);
            if (sg == 0 && cl > 0) fb[0] = ldB(cl, 0);
#pragma unroll
            for (int m = 7 - sg; m >= 0; --m) acc[sg + m] = MFMA32(fa[i % 3], fb[m], acc[sg + m]);
            SB();
        }
    }
    STAMP(4);
    __syncthreads();
    STAMP(6);
    if (uu == 0) a_finish<0>(lds, c, lane, bf2f(r_ds), acc); else a_finish<1>(lds, c, lane, bf2f(r_ds), acc);
    STAMP(8);
    __syncthreads();
    STAMP(9);
    if (w < 8) {
        u32x2 y[6];
#pragma unroll
        for (int ch = 0; ch < 6; ++ch) y[ch] = *(const u32x2*)(lds + A_Y + ch * 4096 + tid * 8);
        char* ob = (char*)p.out + (size_t)(4 * tid) * 2304 + wg * 12;
#pragma unroll
        for (int i = 0; i < 4; ++i) {
            const u32 sel = (i & 1) ? 0x07060302u : 0x05040100u;
            *(u32x3*)(ob + i * 2304) = (u32x3){__builtin_amdgcn_perm(y[1][i >> 1], y[0][i >> 1], sel), __builtin_amdgcn_perm(y[3][i >> 1], y[2][i >> 1], sel), __builtin_amdgcn_perm(y[5][i >> 1], y[4][i >> 1], sel)};
        }
    }
    if (tid == 0) __hip_atomic_store((GAS u32*)(p.ws + WS_STARTED + blockIdx.x * 8), mk_tag(tagv), RLX);
    STAMP(5);
}

__global__ void __launch_bounds__(768, 3) fused_kernel(Params p) {
    extern __shared__ __attribute__((aligned(16))) char lds[];
    const int w = __builtin_amdgcn_readfirstlane(threadIdx.x >> 6);
    const int lane = threadIdx.x & 63;
    const int b = blockIdx.x, xcd = b & 7, r = b >> 3;
    STAMP(0);
    if (r >= 4 && r < 20) {
        const int rr = r - 4, it = 2 * xcd + (rr >> 3), g = rr & 7;
        u32 tagv;
        if (w >= 8) b_helper(p, lds, w - 8, g, it, g, tagv ST_PASS);
        else if (w < 4) b_compute<0>(p, lds, w, g, it, tagv ST_PASS);
        else b_compute<1>(p, lds, w, g, it, tagv ST_PASS);
        STAMP(7);
        b2_tile(p, lds, w, it, g, mk_tag(tagv) ST_PASS);
        STAMP(11);
    } else if (r < 4) {
        u32 tagv;
        spare_body(p, lds, xcd, r, w, tagv ST_PASS);
        if (b == 0 && w == 0) {
            const u32 tag = mk_tag(tagv);
            GAS u32* st = (GAS u32*)(p.ws + WS_STARTED);
            for (int spin = 0; spin < SPIN_CAP; ++spin) {
                bool ok = true;
#pragma unroll
                for (int k = 0; k < 4; ++k) ok &= __hip_atomic_load(st + 2 * (lane + 64 * k), RLX) == tag;
                if (__all(ok)) break;
                __builtin_amdgcn_s_sleep(8);
            }
            if (lane == 0) __hip_atomic_store((GAS u32*)(p.ws + WS_EPOCH), tag, RLX);
        }
    } else {
        u32 tagv;
        a_body(p, lds, xcd * 12 + (r - 20), w, tagv ST_PASS);
    }
}

extern "C" void kernel_launch(void* const* d_in, const int* in_sizes, int n_in, void* d_out, int out_size, void* d_ws, size_t ws_size, hipStream_t stream) {
    Params p;
    p.u = (const u16*)d_in[0]; p.logA = (const u16*)d_in[1]; p.Aim = (const u16*)d_in[2]; p.Cre = (const u16*)d_in[3]; p.Cim = (const u16*)d_in[4];
    p.logdt = (const u16*)d_in[5]; p.Dskip = (const u16*)d_in[6]; p.x = (const u16*)d_in[7]; p.wqkv = (const u16*)d_in[8]; p.wo = (const u16*)d_in[9];
    p.out = (u16*)d_out; p.ws = (char*)d_ws;
    (void)hipFuncSetAttribute((const void*)fused_kernel, hipFuncAttributeMaxDynamicSharedMemorySize, LDS_B);
    fused_kernel<<<256, 768, LDS_B, stream>>>(p);
}
```
